```python
import torch

def _softmax(x, axis=-1):
    return torch.softmax(x, dim=axis)
def _gelu(x):
    return torch.nn.functional.gelu(x, approximate="tanh")
def _silu(x):
    return torch.nn.functional.silu(x)
def _sigmoid(x):
    return torch.sigmoid(x)
def _layernorm(x, axis=-1, eps=1e-5):
    m = x.mean(dim=axis, keepdim=True)
    v = x.var(dim=axis, keepdim=True, unbiased=False)
    return (x - m) / torch.sqrt(v + eps)
def _scatter_add(tgt, idx, src, axis=0):
    return tgt.clone().index_add_(axis, idx, src)
def _segment_sum(src, seg, n):
    out = torch.zeros((n,) + src.shape[1:], dtype=src.dtype)
    return out.index_add_(0, seg, src)
def _pad(x, pw):
    flat = tuple(v for pair in reversed(pw) for v in pair)
    return torch.nn.functional.pad(x, flat)

kernel_name = 'standalone__molecular_angular_gemnet__graph'

def setup_inputs():

    _w = (lambda a: (lambda t: t.to(torch.bfloat16) if t.is_floating_point() else t)(torch.as_tensor(a)))
    N, deg, De, L = 25000, 16, 4, 3
    E = N * deg
    T = E * deg
    NR = 8
    dist = torch.rand(E).to(float)
    angle = (torch.rand(T) * torch.pi).to(float)
    centers = torch.linspace(0.0, 1.0, NR).to(float)
    ang_freq = torch.arange(1, L + 1).to(float)
    return {
        "m": _w((lambda *s: torch.randn(*s, dtype=torch.bfloat16) * 0.05)(E, De)),
        "idx_in": _w((lambda lo, hi, *s: torch.randint(lo, hi, s))(0, E, T).to(torch.int64)),
        "idx_out": _w((lambda lo, hi, *s: torch.randint(lo, hi, s))(0, E, T).to(torch.int64)),
        "dist": _w(dist),
        "angle": _w(angle),
        "centers": _w(centers),
        "ang_freq": _w(ang_freq),
        "w_rbf": _w((lambda *s: torch.randn(*s, dtype=torch.bfloat16) * 0.4)(NR, De)),
        "w_sbf": _w((lambda *s: torch.randn(*s, dtype=torch.bfloat16) * 0.4)(NR * L, De)),
        "w_down": _w((lambda *s: torch.randn(*s, dtype=torch.bfloat16) * 0.4)(De, De)),
        "w_up": _w((lambda *s: torch.randn(*s, dtype=torch.bfloat16) * 0.4)(De, De)),
    }

def reference(m, idx_in, idx_out, dist, angle, centers, ang_freq, w_rbf, w_sbf, w_down, w_up):
    E, De = m.shape[0], m.shape[1]
    NR = centers.shape[0]
    L = ang_freq.shape[0]
    rbf = torch.exp(-10.0 * (dist[:, None] - centers[None, :]) ** 2)
    cbf = torch.cos(ang_freq[None, :] * angle[:, None])
    rbf_t = rbf[idx_out]
    sbf = torch.reshape(rbf_t[:, :, None] * cbf[:, None, :], (-1, NR * L))
    x = (m[idx_in] @ w_down) * (sbf @ w_sbf)
    agg = _segment_sum(x, idx_out, E)
    return m + (agg @ w_up) * (rbf @ w_rbf)
```

```cpp
#include <hip/hip_runtime.h>
#include <hip/hip_bf16.h>
#include <stdint.h>

#define MIMIC 1

__device__ __forceinline__ float bf2f(uint16_t v) { return __uint_as_float(((uint32_t)v) << 16); }
__device__ __forceinline__ float rbf16(float x) {
    return __bfloat162float(__float2bfloat16(x));
}

__global__ void k_zero(float* agg, int n) {
    int i = blockIdx.x * blockDim.x + threadIdx.x;
    if (i < n) agg[i] = 0.f;
}

__global__ void k_trip(const uint16_t* m, const int* idx_in, const int* idx_out, const uint16_t* dist,
                       const uint16_t* angle, const uint16_t* centers, const uint16_t* ang_freq,
                       const uint16_t* w_sbf, const uint16_t* w_down, float* agg, int T) {
    int t = blockIdx.x * blockDim.x + threadIdx.x;
    if (t >= T) return;
    int ei = idx_in[t], eo = idx_out[t];
    float a = bf2f(angle[t]);
    float d = bf2f(dist[eo]);
    float rb[8], cb[3];
    for (int r = 0; r < 8; r++) {
        float df = rbf16(d - bf2f(centers[r]));
        float d2 = rbf16(df * df);
        rb[r] = rbf16(expf(rbf16(-10.f * d2)));
    }
    for (int l = 0; l < 3; l++) cb[l] = rbf16(cosf(rbf16(bf2f(ang_freq[l]) * a)));
    float s[4] = {0, 0, 0, 0};
    for (int r = 0; r < 8; r++)
        for (int l = 0; l < 3; l++) {
            float sb = rb[r] * cb[l];
            for (int c = 0; c < 4; c++) s[c] += sb * bf2f(w_sbf[(r * 3 + l) * 4 + c]);
        }
    float mm[4];
    for (int k = 0; k < 4; k++) mm[k] = bf2f(m[ei * 4 + k]);
    for (int c = 0; c < 4; c++) {
        float md = 0;
        for (int k = 0; k < 4; k++) md += mm[k] * bf2f(w_down[k * 4 + c]);
        atomicAdd(&agg[eo * 4 + c], rbf16(md) * rbf16(s[c]));
    }
}

__global__ void k_fin(const uint16_t* m, const uint16_t* dist, const uint16_t* centers, const uint16_t* w_rbf,
                      const uint16_t* w_up, const float* agg, __hip_bfloat16* out, int E) {
    int e = blockIdx.x * blockDim.x + threadIdx.x;
    if (e >= E) return;
    float d = bf2f(dist[e]);
    float rb[8];
    for (int r = 0; r < 8; r++) {
        float df = rbf16(d - bf2f(centers[r]));
        float d2 = rbf16(df * df);
        rb[r] = rbf16(expf(rbf16(-10.f * d2)));
    }
    for (int c = 0; c < 4; c++) {
        float u = 0, g = 0;
        for (int k = 0; k < 4; k++) u += rbf16(agg[e * 4 + k]) * bf2f(w_up[k * 4 + c]);
        for (int r = 0; r < 8; r++) g += rb[r] * bf2f(w_rbf[r * 4 + c]);
        out[e * 4 + c] = __float2bfloat16(bf2f(m[e * 4 + c]) + rbf16(rbf16(u) * rbf16(g)));
    }
}

extern "C" void kernel_launch(void* const* d_in, const int* in_sizes, int n_in,
                              void* d_out, int out_size, void* d_ws, size_t ws_size,
                              hipStream_t stream) {
    const uint16_t* m = (const uint16_t*)d_in[0];
    const int* idx_in = (const int*)d_in[1];
    const int* idx_out = (const int*)d_in[2];
    const uint16_t* dist = (const uint16_t*)d_in[3];
    const uint16_t* angle = (const uint16_t*)d_in[4];
    const uint16_t* centers = (const uint16_t*)d_in[5];
    const uint16_t* ang_freq = (const uint16_t*)d_in[6];
    const uint16_t* w_rbf = (const uint16_t*)d_in[7];
    const uint16_t* w_sbf = (const uint16_t*)d_in[8];
    const uint16_t* w_down = (const uint16_t*)d_in[9];
    const uint16_t* w_up = (const uint16_t*)d_in[10];
    int E = in_sizes[3], T = in_sizes[1];
    float* agg = (float*)d_ws;
    k_zero<<<(E * 4 + 255) / 256, 256, 0, stream>>>(agg, E * 4);
    k_trip<<<(T + 255) / 256, 256, 0, stream>>>(m, idx_in, idx_out, dist, angle, centers, ang_freq, w_sbf, w_down, agg, T);
    k_fin<<<(E + 255) / 256, 256, 0, stream>>>(m, dist, centers, w_rbf, w_up, agg, (__hip_bfloat16*)d_out, E);
}
```

```cpp
#include <hip/hip_runtime.h>
#include <hip/hip_bf16.h>
#include <stdint.h>

typedef unsigned int u32;
typedef unsigned short u16;
typedef unsigned long long u64;
typedef u32 u32x2 __attribute__((ext_vector_type(2)));
typedef u32 u32x3 __attribute__((ext_vector_type(3)));
typedef u32 u32x4 __attribute__((ext_vector_type(4)));
typedef _Float16 h16x4 __attribute__((ext_vector_type(4)));
typedef _Float16 h16x8 __attribute__((ext_vector_type(8)));
typedef __bf16 bf16x2 __attribute__((ext_vector_type(2)));

#define P1_THREADS 512
#define P1_QUADS 7
#define NCHUNK 512
#define ST_POLICY 2
constexpr int NB = 256;
constexpr int NLOC = 1600;
constexpr int OFF_STRIDE = 320;

__device__ __forceinline__ float bf_lo(u32 w) { return __uint_as_float(w << 16); }
__device__ __forceinline__ float bf_hi(u32 w) { return __uint_as_float(w & 0xffff0000u); }
__device__ __forceinline__ float bf2f(u16 v) { return __uint_as_float(((u32)v) << 16); }
__device__ __forceinline__ u32 pk_bf16(float a, float b) { bf16x2 v = {(__bf16)a, (__bf16)b}; return __builtin_bit_cast(u32, v); }
__device__ __forceinline__ float rb16(float x) { return bf_lo(pk_bf16(x, x)); }

__device__ __forceinline__ u32 wave_incl_scan(u32 x) {
    x += __builtin_amdgcn_update_dpp(0, x, 0x111, 0xf, 0xf, false);
    x += __builtin_amdgcn_update_dpp(0, x, 0x112, 0xf, 0xf, false);
    x += __builtin_amdgcn_update_dpp(0, x, 0x114, 0xf, 0xf, false);
    x += __builtin_amdgcn_update_dpp(0, x, 0x118, 0xf, 0xf, false);
    x += __builtin_amdgcn_update_dpp(0, x, 0x142, 0xa, 0xf, false);
    x += __builtin_amdgcn_update_dpp(0, x, 0x143, 0xc, 0xf, false);
    return x;
}
__device__ __forceinline__ u32 wave_max_u32(u32 x) {
    x = max(x, (u32)__builtin_amdgcn_update_dpp(0, x, 0x111, 0xf, 0xf, false));
    x = max(x, (u32)__builtin_amdgcn_update_dpp(0, x, 0x112, 0xf, 0xf, false));
    x = max(x, (u32)__builtin_amdgcn_update_dpp(0, x, 0x114, 0xf, 0xf, false));
    x = max(x, (u32)__builtin_amdgcn_update_dpp(0, x, 0x118, 0xf, 0xf, false));
    x = max(x, (u32)__builtin_amdgcn_update_dpp(0, x, 0x142, 0xa, 0xf, false));
    x = max(x, (u32)__builtin_amdgcn_update_dpp(0, x, 0x143, 0xc, 0xf, false));
    return __builtin_amdgcn_readlane(x, 63);
}
__device__ __forceinline__ void store16(u32x4 v, void* p) {
    asm volatile("global_store_dwordx4 %0, %1, off sc1" :: "v"(p), "v"(v) : "memory");
}

#define STAMP(dbg, i) do {} while (0)
#define P1_MINW 4
__global__ void __launch_bounds__(P1_THREADS, P1_MINW) k_bin(const u32x4* __restrict__ idx_in, const u32x4* __restrict__ idx_out,
        const u32x2* __restrict__ angle, const u32x2* __restrict__ m, const u32* __restrict__ w_down,
        char* __restrict__ recs, u16* __restrict__ offtab, u32* __restrict__ stats,
        int T, int E, int C, int rec_stride, int rows_per_chunk) {
    extern __shared__ __attribute__((aligned(16))) char smem[];
    u32* cnt = (u32*)smem;
    u16* off = (u16*)(smem + NB * 4);
    u32* misc = (u32*)(smem + NB * 6);
    char* stage = smem + NB * 6 + 16;
    const int tid = threadIdx.x, c = blockIdx.x;
    const int base = c * C;
    const int n = min(C, T - base);
    const int nq = n >> 2;
    const int qbase = base >> 2;
    u64* dbg = (u64*)(recs + (208u << 20));
    STAMP(dbg, 0);

    constexpr int MR = 2048 / P1_THREADS;
    u32x2 mv[MR];
    {
        int r0 = c * rows_per_chunk;
        int nr = max(0, min(rows_per_chunk, E - r0));
        auto rm = __builtin_amdgcn_make_buffer_rsrc((void*)(m + r0), 0, nr * 8, 0x00020000);
#pragma unroll
        for (int j = 0; j < MR; j++) mv[j] = __builtin_amdgcn_raw_buffer_load_b64(rm, tid * 8, j * P1_THREADS * 8, 0);
    }
    u32x4 vo[P1_QUADS], vi[P1_QUADS];
    u32x2 va[P1_QUADS];
    {
        auto ro = __builtin_amdgcn_make_buffer_rsrc((void*)(idx_out + qbase), 0, nq * 16, 0x00020000);
        auto ri = __builtin_amdgcn_make_buffer_rsrc((void*)(idx_in + qbase), 0, nq * 16, 0x00020000);
        auto ra = __builtin_amdgcn_make_buffer_rsrc((void*)(angle + qbase), 0, nq * 8, 0x00020000);
#pragma unroll
        for (int q = 0; q < P1_QUADS; q++) vo[q] = __builtin_amdgcn_raw_buffer_load_b128(ro, tid * 16, q * P1_THREADS * 16, 2);
#pragma unroll
        for (int q = 0; q < P1_QUADS; q++) {
            vi[q] = __builtin_amdgcn_raw_buffer_load_b128(ri, tid * 16, q * P1_THREADS * 16, 2);
            va[q] = __builtin_amdgcn_raw_buffer_load_b64(ra, tid * 8, q * P1_THREADS * 8, 2);
        }
    }
    if (tid < NB) cnt[tid] = 0;
    if (tid < 4) misc[tid] = 0;
    __syncthreads();
    {
        u32 mmax = 0;
        float wd[16];
#pragma unroll
        for (int k = 0; k < 8; k++) { u32 w = w_down[k]; wd[2 * k] = bf_lo(w); wd[2 * k + 1] = bf_hi(w); }
#pragma unroll
        for (int j = 0; j < MR; j++) {
            u32x2 v = mv[j];
            float a0 = bf_lo(v.x), a1 = bf_hi(v.x), a2 = bf_lo(v.y), a3 = bf_hi(v.y);
#pragma unroll
            for (int d = 0; d < 4; d++) {
                float x = a0 * wd[d] + a1 * wd[4 + d] + a2 * wd[8 + d] + a3 * wd[12 + d];
                mmax = max(mmax, __float_as_uint(x) & 0x7fffffffu);
            }
        }
        mmax = wave_max_u32(mmax);
        if ((tid & 63) == 0) atomicMax(&misc[0], mmax);
    }
    STAMP(dbg, 1);
    u32 rk[P1_QUADS][4];
#pragma unroll
    for (int q = 0; q < P1_QUADS; q++) {
        int i = tid + q * P1_THREADS;
        if (i < nq) {
#pragma unroll
            for (int j = 0; j < 4; j++) rk[q][j] = atomicAdd(&cnt[(vo[q][j] >> 4) & 255], 1u);
        }
    }
    STAMP(dbg, 2);
    __syncthreads();
    STAMP(dbg, 3);
    {
        int lane = tid & 63;
        u32x4 cv = *(const u32x4*)(cnt + lane * 4);
        u32 s = cv.x + cv.y + cv.z + cv.w;
        u32 ex = wave_incl_scan(s) - s;
        u32 o0 = ex, o1 = ex + cv.x, o2 = o1 + cv.y, o3 = o2 + cv.z;
        u32x2 pk = {o0 | (o1 << 16), o2 | (o3 << 16)};
        *(u32x2*)(off + lane * 4) = pk;
        if (tid < 64) {
            *(u32x2*)(offtab + (size_t)c * OFF_STRIDE + lane * 4) = pk;
            if (tid == 0) { offtab[(size_t)c * OFF_STRIDE + NB] = (u16)n; stats[c] = misc[0]; }
        }
        __builtin_amdgcn_wave_barrier();
    }
    STAMP(dbg, 4);
#pragma unroll
    for (int q = 0; q < P1_QUADS; q++) {
        int i = tid + q * P1_THREADS;
        if (i < nq) {
#pragma unroll
            for (int j = 0; j < 4; j++) {
                u32 o = vo[q][j];
                u32 pos = off[(o >> 4) & 255] + rk[q][j];
                u32 loc = ((o >> 8) & ~15u) | (o & 15u);
                u32 word = vi[q][j] | (loc << 19);
                u32 odd = pos & 1;
                u32 a6 = pos * 6;
                *(u32*)(stage + a6 - odd * 2) = word;
                u32 ang = (j & 1) ? (va[q][j >> 1] >> 16) : va[q][j >> 1];
                *(u16*)(stage + a6 + 8 - odd * 4) = (u16)ang;
            }
        }
    }
    STAMP(dbg, 5);
    __syncthreads();
    STAMP(dbg, 6);
    const int n16 = (((n + 1) >> 1) * 12 + 15) >> 4;
    char* dst = recs + (size_t)c * rec_stride;
    for (int i = tid; i < n16; i += P1_THREADS) store16(*(const u32x4*)(stage + i * 16), dst + i * 16);
    STAMP(dbg, 7);
    STAMP(dbg, 8);
}

constexpr int LCAP = 14336;
constexpr int L_GA = 0;
constexpr int L_GB = L_GA + NLOC * 16;
constexpr int L_A0 = L_GB + NLOC * 8;
constexpr int L_A1 = L_A0 + NLOC * 8;
constexpr int L_LIST = L_A1 + NLOC * 8;
constexpr int L_MISC = L_LIST + LCAP * 4;
constexpr int L_TOTAL = L_MISC + 128;

struct Consts { float fr[3]; float wd[16]; };

__device__ __forceinline__ void process(char* smem, const Consts& k, u32 word, u32 angbits  , u32x2 g) {
    float a = __uint_as_float(angbits);
    u32 p01 = pk_bf16(k.fr[0] * a, k.fr[1] * a);
    u32 p2 = pk_bf16(k.fr[2] * a, 0.f);
    const float inv2pi = 0.15915494309189535f;
    float c0 = __builtin_amdgcn_cosf(__builtin_amdgcn_fractf(bf_lo(p01) * inv2pi));
    float c1 = __builtin_amdgcn_cosf(__builtin_amdgcn_fractf(bf_hi(p01) * inv2pi));
    float c2 = __builtin_amdgcn_cosf(__builtin_amdgcn_fractf(bf_lo(p2) * inv2pi));
    u32 l8 = (word >> 19) << 3;
    h16x8 ga = *(const h16x8*)(smem + L_GA + l8 * 2);
    h16x4 gb = *(const h16x4*)(smem + L_GB + l8);
    float a0 = bf_lo(g.x), a1 = bf_hi(g.x), a2 = bf_lo(g.y), a3 = bf_hi(g.y);
    u32 q[4];
#pragma unroll
    for (int d = 0; d < 4; d++) {
        float s = __builtin_fmaf((float)gb[d], c2, __builtin_fmaf((float)ga[4 + d], c1, (float)ga[d] * c0));
        float md = __builtin_fmaf(a3, k.wd[12 + d], __builtin_fmaf(a2, k.wd[8 + d], __builtin_fmaf(a1, k.wd[4 + d], a0 * k.wd[d])));
        q[d] = __float_as_uint(__builtin_fmaf(md, s, 12582912.0f));
    }
    u64 w0 = (u64)((q[0] << 8) | 1u) | ((u64)(q[1] << 4) << 32);
    u64 w1 = (u64)q[2] | ((u64)q[3] << 32);
    atomicAdd((u64*)(smem + L_A0 + l8), w0);
    atomicAdd((u64*)(smem + L_A1 + l8), w1);
}

#define PF 4
#define GD 2

#define BURST 0
__device__ __forceinline__ float wsel(const u32* __restrict__ w, int i) { u32 x = w[i >> 1]; return (i & 1) ? bf_hi(x) : bf_lo(x); }

__global__ void __launch_bounds__(1024) k_acc(const u32x2* __restrict__ m, const u16* __restrict__ dist, const u32* __restrict__ centers,
        const u16* __restrict__ ang_freq, const u32* __restrict__ w_rbf, const u32* __restrict__ w_sbf, const u32* __restrict__ w_down,
        const u32* __restrict__ w_up, const char* __restrict__ recs, const u16* __restrict__ offtab, const u32* __restrict__ stats,
        u32x2* __restrict__ out, int E, int NC, int rec_stride) {
    extern __shared__ __attribute__((aligned(16))) char smem[];
    u32* list = (u32*)(smem + L_LIST);
    u32* misc = (u32*)(smem + L_MISC);
    const int tid = threadIdx.x, lane = tid & 63, wave = tid >> 6;
    const int b = (blockIdx.x & 7) * 32 + (blockIdx.x >> 3);
    u64* dbg = (u64*)(recs + (200u << 20));
    STAMP(dbg, 0);
    auto rM = __builtin_amdgcn_make_buffer_rsrc((void*)m, 0, E * 8, 0x00020000);
    auto rD = __builtin_amdgcn_make_buffer_rsrc((void*)dist, 0, E * 2, 0x00020000);
    auto rR = __builtin_amdgcn_make_buffer_rsrc((void*)recs, 0, NC * rec_stride, 0x00020000);
    auto rO = __builtin_amdgcn_make_buffer_rsrc((void*)offtab, 0, NC * OFF_STRIDE * 2, 0x00020000);
    auto rS = __builtin_amdgcn_make_buffer_rsrc((void*)stats, 0, NC * 4, 0x00020000);

    u32 o0 = __builtin_amdgcn_raw_buffer_load_b16(rO, tid * OFF_STRIDE * 2, b * 2, 0);
    u32 o1 = __builtin_amdgcn_raw_buffer_load_b16(rO, tid * OFF_STRIDE * 2, b * 2 + 2, 0);
    u32 st = __builtin_amdgcn_raw_buffer_load_b32(rS, tid * 4, 0, 0);
    u32 dv[2]; u32x2 mrow[2];
#pragma unroll
    for (int j = 0; j < 2; j++) {
        int loc = tid + j * 1024;
        int e = ((loc >> 4) << 12) | (b << 4) | (loc & 15);
        dv[j] = __builtin_amdgcn_raw_buffer_load_b16(rD, e * 2, 0, 0);
        mrow[j] = __builtin_amdgcn_raw_buffer_load_b64(rM, e * 8, 0, 0);
    }
    float fr0 = bf2f(ang_freq[0]), fr1 = bf2f(ang_freq[1]), fr2 = bf2f(ang_freq[2]);
    u32 nrec = o1 - o0;
    u32 u0 = o0 >> 1;
    u32 nu = nrec ? (((o1 - 1) >> 1) - u0 + 1) : 0;
    u32 inc = wave_incl_scan(nu);
    u32 wmax = wave_max_u32(st);
    if (lane == 63) { misc[wave] = inc; misc[16 + wave] = wmax; }
    STAMP(dbg, 1);
    __syncthreads();
    u32 wt = lane < 16 ? misc[lane] : 0;
    u32 mx = lane < 16 ? misc[16 + lane] : 0;
    u32 wi = wave_incl_scan(wt);
    u32 total = __builtin_amdgcn_readlane(wi, 15);
    u32 wbase = __builtin_amdgcn_readlane(wi - wt, wave);
    mx = wave_max_u32(mx);
    {
        u32 pos = wbase + inc - nu;
        u32 ent = (((u32)tid * (rec_stride / 4) + u0 * 3) << 2) | 3;
        u32 lim = min(nu, pos < LCAP ? LCAP - pos : 0u);
#pragma nounroll
        for (u32 j = 0; j < lim; j++) {
            u32 e = ent + j * 12;
            if (j == 0 && (o0 & 1)) e &= ~1u;
            if (j == nu - 1 && (o1 & 1)) e &= ~2u;
            list[pos + j] = e;
        }
    }
    total = min(total, (u32)LCAP);
    for (u32 i = total + tid; i < LCAP; i += 1024) list[i] = 0xfffffff0u;
    __syncthreads();
    STAMP(dbg, 2);
    constexpr int NIT = LCAP / 1024;
    u32x3 r[NIT]; u32 fl = 0;
#pragma unroll
    for (int u = 0; u < PF; u++) { u32 en = list[tid + u * 1024]; fl |= (en & 3) << (2 * u); r[u] = __builtin_amdgcn_raw_buffer_load_b96(rR, en & ~3u, 0, 2); }
    float S[2][4], R[2][4];
#pragma unroll
    for (int j = 0; j < 2; j++) {
        int loc = tid + j * 1024;
        if (loc < NLOC) {
            float d = __uint_as_float(dv[j] << 16);
            float G[12];
#pragma unroll
            for (int i = 0; i < 12; i++) G[i] = 0.f;
#pragma unroll
            for (int i = 0; i < 4; i++) R[j][i] = 0.f;
#pragma unroll
            for (int r = 0; r < 8; r++) {
                float df = rb16(d - wsel(centers, r));
                float rb = rb16(__expf(rb16(-10.f * rb16(df * df))));
#pragma unroll
                for (int i = 0; i < 12; i++) G[i] = __builtin_fmaf(rb, wsel(w_sbf, r * 12 + i), G[i]);
#pragma unroll
                for (int i = 0; i < 4; i++) R[j][i] = __builtin_fmaf(rb, wsel(w_rbf, r * 4 + i), R[j][i]);
            }
            h16x8 ga; h16x4 gb;
#pragma unroll
            for (int d4 = 0; d4 < 4; d4++) {
                float s = fabsf(G[d4]) + fabsf(G[4 + d4]) + fabsf(G[8 + d4]);
                float inv = s > 0.f ? __builtin_amdgcn_rcpf(s) : 0.f;
                S[j][d4] = s;
                ga[d4] = (_Float16)(G[d4] * inv); ga[4 + d4] = (_Float16)(G[4 + d4] * inv); gb[d4] = (_Float16)(G[8 + d4] * inv);
            }
            *(h16x8*)(smem + L_GA + loc * 16) = ga;
            *(h16x4*)(smem + L_GB + loc * 8) = gb;
            *(u64*)(smem + L_A0 + loc * 8) = 0;
            *(u64*)(smem + L_A1 + loc * 8) = 0;
        }
    }
    Consts k;
    int ex = (int)(mx >> 23) + 1;
    float K = __uint_as_float((u32)(127 + 19 + 127 - ex) << 23);
    float Kinv = __uint_as_float((u32)(ex - 19) << 23);
    k.fr[0] = fr0; k.fr[1] = fr1; k.fr[2] = fr2;
#pragma unroll
    for (int i = 0; i < 16; i++) k.wd[i] = __uint_as_float(__builtin_amdgcn_readfirstlane(__float_as_uint(wsel(w_down, i) * K)));

    STAMP(dbg, 3);
    u32x2 g0[NIT], g1[NIT];
    auto gather = [&](int u) {
        u32 a0 = (fl >> (2 * u)) & 1 ? (r[u].x & 0x7ffff) * 8 : 0xfffffff0u;
        u32 a1 = (fl >> (2 * u + 1)) & 1 ? (r[u].y & 0x7ffff) * 8 : 0xfffffff0u;
        g0[u] = __builtin_amdgcn_raw_buffer_load_b64(rM, a0, 0, 0);
        g1[u] = __builtin_amdgcn_raw_buffer_load_b64(rM, a1, 0, 0);
    };
#pragma unroll
    for (int u = 0; u < GD; u++) gather(u);
    asm volatile("s_waitcnt lgkmcnt(0)" ::: "memory");
    __builtin_amdgcn_s_barrier();
    asm volatile("" ::: "memory");
    STAMP(dbg, 4);
#pragma unroll
    for (int u = 0; u < NIT; u++) {
        if (u + PF < NIT) { u32 en = list[tid + (u + PF) * 1024]; fl |= (en & 3) << (2 * (u + PF)); r[u + PF] = __builtin_amdgcn_raw_buffer_load_b96(rR, en & ~3u, 0, 2); }
        if (u + GD < NIT) gather(u + GD);
        if ((fl >> (2 * u)) & 1) process(smem, k, r[u].x, r[u].z << 16, g0[u]);
        if ((fl >> (2 * u + 1)) & 1) process(smem, k, r[u].y, r[u].z & 0xffff0000u, g1[u]);
        if (u == 3) STAMP(dbg, 5);
        if (u == 7) STAMP(dbg, 6);
    }
    STAMP(dbg, 7);
    __syncthreads();
    STAMP(dbg, 8);
#pragma unroll
    for (int j = 0; j < 2; j++) {
        int loc = tid + j * 1024;
        int e = ((loc >> 4) << 12) | (b << 4) | (loc & 15);
        if (loc < NLOC && e < E) {
            u64 w0 = *(const u64*)(smem + L_A0 + loc * 8), w1 = *(const u64*)(smem + L_A1 + loc * 8);
            u64 n = w0 & 255;
            w0 -= n * (0x40000001ull + (0xB4000000ull << 32));
            w1 -= n * (0x4B400000ull + (0x4B400000ull << 32));
            long long f0 = ((long long)(w0 << 28)) >> 36;
            long long f1 = ((long long)(w0 - ((u64)f0 << 8))) >> 36;
            int f2 = (int)(u32)w1;
            int f3 = (int)((long long)(w1 - (u64)(long long)f2) >> 32);
            float ag[4] = {(float)(int)f0 * S[j][0] * Kinv, (float)(int)f1 * S[j][1] * Kinv, (float)f2 * S[j][2] * Kinv, (float)f3 * S[j][3] * Kinv};
            float mm[4] = {bf_lo(mrow[j].x), bf_hi(mrow[j].x), bf_lo(mrow[j].y), bf_hi(mrow[j].y)};
            float o[4];
#pragma unroll
            for (int d = 0; d < 4; d++) {
                float u = ag[0] * wsel(w_up, d) + ag[1] * wsel(w_up, 4 + d) + ag[2] * wsel(w_up, 8 + d) + ag[3] * wsel(w_up, 12 + d);
                o[d] = mm[d] + u * R[j][d];
            }
            out[e] = (u32x2){pk_bf16(o[0], o[1]), pk_bf16(o[2], o[3])};
        }
    }
    STAMP(dbg, 9);
}

extern "C" void kernel_launch(void* const* d_in, const int* in_sizes, int n_in,
                              void* d_out, int out_size, void* d_ws, size_t ws_size,
                              hipStream_t stream) {
    const int E = in_sizes[3], T = in_sizes[1];
    int C = ((T + NCHUNK - 1) / NCHUNK + 3) & ~3;
    int NC = (T + C - 1) / C;
    int rec_stride = ((C / 2) * 12 + 127) / 128 * 128;
    char* ws = (char*)d_ws;
    u32* stats = (u32*)ws;
    u16* offtab = (u16*)(ws + 8192);
    char* recs = ws + (1 << 20);
    int rows = (E + NC - 1) / NC;
    size_t lds1 = NB * 6 + 16 + rec_stride;
    (void)hipFuncSetAttribute((const void*)k_bin, hipFuncAttributeMaxDynamicSharedMemorySize, (int)lds1);
    (void)hipFuncSetAttribute((const void*)k_acc, hipFuncAttributeMaxDynamicSharedMemorySize, L_TOTAL);
    k_bin<<<NC, P1_THREADS, lds1, stream>>>((const u32x4*)d_in[1], (const u32x4*)d_in[2], (const u32x2*)d_in[4], (const u32x2*)d_in[0],
        (const u32*)d_in[9], recs, offtab, stats, T, E, C, rec_stride, rows);
    k_acc<<<NB, 1024, L_TOTAL, stream>>>((const u32x2*)d_in[0], (const u16*)d_in[3], (const u32*)d_in[5], (const u16*)d_in[6],
        (const u32*)d_in[7], (const u32*)d_in[8], (const u32*)d_in[9], (const u32*)d_in[10], recs, offtab, stats, (u32x2*)d_out, E, NC, rec_stride);
}
```

```cpp
#include <hip/hip_runtime.h>
#include <hip/hip_bf16.h>
#include <stdint.h>

typedef unsigned int u32;
typedef unsigned short u16;
typedef unsigned long long u64;
typedef u32 u32x2 __attribute__((ext_vector_type(2)));
typedef u32 u32x3 __attribute__((ext_vector_type(3)));
typedef u32 u32x4 __attribute__((ext_vector_type(4)));
typedef unsigned short u16x2 __attribute__((ext_vector_type(2)));
typedef short s16x4 __attribute__((ext_vector_type(4)));
typedef float f32x4 __attribute__((ext_vector_type(4)));
typedef _Float16 h16x2 __attribute__((ext_vector_type(2)));
typedef __bf16 bf16x2 __attribute__((ext_vector_type(2)));
typedef __amdgpu_buffer_rsrc_t rsrc_t;

constexpr int NB = 256;
constexpr int H = 4;
constexpr int NBIN = 56;
constexpr int BSTR = 131;
constexpr int NK = NB * H;
constexpr int NLOC = 1568;
constexpr int QA = 4, QB = 3;
constexpr int A_BYTES = QA * 4096 * 6;
constexpr int OOB = 0x7ffffff0;
constexpr int AUX_NT = 2, AUX_SC1 = 16;
constexpr int CODE_PF = 22528;

__device__ __forceinline__ float bf_lo(u32 w) { return __uint_as_float(w << 16); }
__device__ __forceinline__ float bf_hi(u32 w) { return __uint_as_float(w & 0xffff0000u); }
__device__ __forceinline__ u32 pk_bf16(float a, float b) { bf16x2 v = {(__bf16)a, (__bf16)b}; u32 r = __builtin_bit_cast(u32, v); asm("" : "+v"(r)); return r; }
__device__ __forceinline__ u32 pk_f16(float a, float b) { h16x2 v = {(_Float16)a, (_Float16)b}; return __builtin_bit_cast(u32, v); }
__device__ __forceinline__ rsrc_t mkrsrc(const void* p, int bytes) { return __builtin_amdgcn_make_buffer_rsrc((void*)p, 0, bytes, 0x00020000); }
__device__ __forceinline__ void lds_barrier() { asm volatile("s_waitcnt lgkmcnt(0)\n\ts_barrier" ::: "memory"); }
__device__ __forceinline__ void lds_wait() { asm volatile("s_waitcnt lgkmcnt(0)" ::: "memory"); }
__device__ __forceinline__ void pin() { __builtin_amdgcn_sched_barrier(0); }
__device__ __forceinline__ float dot2bf(u32 a, u32 b, float c) { return __builtin_amdgcn_fdot2_f32_bf16(__builtin_bit_cast(bf16x2, a), __builtin_bit_cast(bf16x2, b), c, false); }
__device__ __forceinline__ u32 mul24(u32 a, u32 b) { return (u32)__umul24(a, b); }
__device__ __forceinline__ u32 pk_max_u16(u32 a, u32 b) { return __builtin_bit_cast(u32, __builtin_elementwise_max(__builtin_bit_cast(u16x2, a), __builtin_bit_cast(u16x2, b))); }
__device__ __forceinline__ u32 row_incl_scan(u32 x) {
    x += __builtin_amdgcn_update_dpp(0, x, 0x111, 0xf, 0xf, false);
    x += __builtin_amdgcn_update_dpp(0, x, 0x112, 0xf, 0xf, false);
    x += __builtin_amdgcn_update_dpp(0, x, 0x114, 0xf, 0xf, false);
    x += __builtin_amdgcn_update_dpp(0, x, 0x118, 0xf, 0xf, false);
    return x;
}
__device__ __forceinline__ u32 wave_incl_scan(u32 x) {
    x = row_incl_scan(x);
    x += __builtin_amdgcn_update_dpp(0, x, 0x142, 0xa, 0xf, false);
    x += __builtin_amdgcn_update_dpp(0, x, 0x143, 0xc, 0xf, false);
    return x;
}
__device__ __forceinline__ u32 wave_max_u32(u32 x) {
    x = max(x, (u32)__builtin_amdgcn_update_dpp(0, x, 0x111, 0xf, 0xf, false));
    x = max(x, (u32)__builtin_amdgcn_update_dpp(0, x, 0x112, 0xf, 0xf, false));
    x = max(x, (u32)__builtin_amdgcn_update_dpp(0, x, 0x114, 0xf, 0xf, false));
    x = max(x, (u32)__builtin_amdgcn_update_dpp(0, x, 0x118, 0xf, 0xf, false));
    x = max(x, (u32)__builtin_amdgcn_update_dpp(0, x, 0x142, 0xa, 0xf, false));
    x = max(x, (u32)__builtin_amdgcn_update_dpp(0, x, 0x143, 0xc, 0xf, false));
    return __builtin_amdgcn_readlane(x, 63);
}
__device__ __forceinline__ float wave_sum_f32(float x) {
#pragma unroll
    for (int o = 32; o > 0; o >>= 1) x += __shfl_xor(x, o);
    return x;
}
__device__ __forceinline__ u32 from_lane(u32 v, u32 l) { return (u32)__builtin_amdgcn_ds_bpermute((int)(l * 4), (int)v); }

#define LDS(T, off) (*(__attribute__((address_space(3))) T*)(uintptr_t)(u32)(off))
__device__ __forceinline__ u32 lds_add_rtn(u32 off, u32 v) { return __hip_atomic_fetch_add(&LDS(u32, off), v, __ATOMIC_RELAXED, __HIP_MEMORY_SCOPE_WORKGROUP); }
__device__ __forceinline__ void lds_add64(u32 off, u64 v) { (void)__hip_atomic_fetch_add(&LDS(u64, off), v, __ATOMIC_RELAXED, __HIP_MEMORY_SCOPE_WORKGROUP); }

template <int DUMMY>
__device__ __forceinline__ void l2_touch(rsrc_t rs, u32 off) {
    u32 keep;
    asm volatile("s_mov_b32 %0, m0\n\ts_mov_b32 m0, %3\n\ts_nop 4\n\tbuffer_load_dword %1, %2, 0 offen sc1 lds\n\ts_mov_b32 m0, %0"
                 : "=&s"(keep) : "v"(off), "s"(rs), "i"(DUMMY));
}

constexpr int LDS_BYTES = 163840;
constexpr int C_W = LDS_BYTES - 512;
constexpr int C_CEN = C_W + 256;
constexpr int C_WD = C_CEN + 32;
constexpr int C_WU = C_WD + 32;
constexpr int C_FR = C_WU + 64;
constexpr int C_ST = C_W - 16;
constexpr int B_CNT = 0;
constexpr int B_MISC = B_CNT + 2 * NK * 4;
constexpr int B_DUMMY = B_MISC + 128;
constexpr int B_STAGE = B_DUMMY + 256;
constexpr int OCAP = 1024;
constexpr int L_GA = 0;
constexpr int L_GB = L_GA + NLOC * 16;
constexpr int L_A0 = L_GB + NLOC * 8;
constexpr int L_A1 = L_A0 + NLOC * 8;
constexpr int L_CNT = L_A1 + NLOC * 8;
constexpr int L_FIN = L_CNT + 256;
constexpr int L_MISC = L_FIN + 256;
constexpr int L_STAGE = L_MISC + 256;
constexpr int L_SR = L_STAGE + (NBIN * BSTR + OCAP) * 8;
constexpr int L_TOTAL = L_SR + NLOC * 16;
constexpr int L_F64 = L_STAGE;
static_assert(NLOC * 32 <= (NBIN * BSTR + OCAP) * 8 && 65536 <= (NBIN * BSTR + OCAP) * 8 && L_TOTAL <= C_ST, "LDS map");
static_assert(L_STAGE < 65536 && L_CNT <= B_STAGE + A_BYTES && B_STAGE <= L_A0 && (B_STAGE & 15) == 0, "LDS map");

struct BinK { u32 MG, pad; };

template <int NQ, int WHICH, typename F>
__device__ __forceinline__ u32 bin_chunk(const u32x4* vo, const u32x4* vi, const u32x2* va, int nq, BinK bk, u32& amax, F&& between) {
    const int tid = threadIdx.x, lane = tid & 63, wave = tid >> 6;
    constexpr int CNT = B_CNT + WHICH * NK * 4, WT = B_MISC + WHICH * 64, STG = B_STAGE + WHICH * A_BYTES;
    u32 key[NQ][4], rk[NQ][4], word[NQ][4];
#pragma unroll
    for (int q = 0; q < NQ; q++) {
        between(q);
        if (tid + q * 1024 < nq) {
#pragma unroll
            for (int j = 0; j < 4; j++) {
                u32 o = vo[q][j], i = vi[q][j];
                u32 gb = mul24(i >> 4, bk.MG) >> 22;
                key[q][j] = (o & 0xff0) | ((gb & 3) << 2);
                u32 loc4 = ((o << 2) & 0x3c) | ((o >> 6) & ~0x3cu);
                word[q][j] = ((i + mul24(gb, bk.pad)) << 13) | loc4;
                rk[q][j] = lds_add_rtn(CNT + key[q][j], 6u);
            }
        }
    }
    between(NQ);
    lds_barrier();
    between(100);
    u32 c = LDS(u32, CNT + tid * 4);
    u32 inc = wave_incl_scan(c);
    if (lane == 63) LDS(u32, WT + wave * 4) = inc;
    lds_barrier();
    between(101);
    u32 wt = LDS(u32, WT + (lane & 15) * 4);
    u32 wb = __builtin_amdgcn_readlane(row_incl_scan(wt) - wt, wave);
    u32 ex = wb + inc - c;
    LDS(u32, CNT + tid * 4) = ex + (STG - 2);
    lds_barrier();
#pragma unroll
    for (int q = 0; q < NQ; q++) {
        between(NQ + 1 + q);
        amax = pk_max_u16(amax, pk_max_u16(va[q].x & 0x7fff7fffu, va[q].y & 0x7fff7fffu));
        if (tid + q * 1024 < nq) {
#pragma unroll
            for (int j = 0; j < 4; j++) {
                u32 a = LDS(u32, CNT + key[q][j]) + rk[q][j];
                LDS(u32, (a & ~3u) + 4) = word[q][j];
                u32 ang = (j & 1) ? (va[q][j >> 1] >> 16) : va[q][j >> 1];
                LDS(u16, a + (a & 2) * 2 + 2) = (u16)ang;
            }
        }
    }
    return (mul24(ex >> 1, 43691) >> 17) | ((mul24((ex + c) >> 1, 43691) >> 17) << 16);
}

struct Consts { float fr[3]; float magic; u32 wd[8]; };

struct GRead { u32 l8; u32x4 ga; u32x2 gb; };
__device__ __forceinline__ GRead g_read(u32 y) {
    GRead o;
    o.l8 = (y & 0x1ffc) * 2;
    o.ga = LDS(u32x4, L_GA + o.l8 * 2);
    o.gb = LDS(u32x2, L_GB + o.l8);
    return o;
}
__device__ __forceinline__ void cos2(const Consts& k, u32 ya, u32 yb, u32& c01a, float& c2a, u32& c01b, float& c2b) {
    const float a = bf_hi(ya), b = bf_hi(yb);
    const float inv2pi = 0.15915494309189535f;
    u32 pa = pk_bf16(k.fr[0] * a, k.fr[1] * a), pb = pk_bf16(k.fr[0] * b, k.fr[1] * b), p2 = pk_bf16(k.fr[2] * a, k.fr[2] * b);
    c01a = pk_f16(__builtin_amdgcn_cosf(bf_lo(pa) * inv2pi), __builtin_amdgcn_cosf(bf_hi(pa) * inv2pi));
    c01b = pk_f16(__builtin_amdgcn_cosf(bf_lo(pb) * inv2pi), __builtin_amdgcn_cosf(bf_hi(pb) * inv2pi));
    c2a = __builtin_amdgcn_cosf(bf_lo(p2) * inv2pi);
    c2b = __builtin_amdgcn_cosf(bf_hi(p2) * inv2pi);
}
__device__ __forceinline__ void accum(const Consts& k, const GRead& gr, u32 c01, float c2, u32x2 g, bool valid) {
    const u32x4 ga = gr.ga;
    const u32x2 gb = gr.gb;
    u32 q0, q1, q2, q3, s0, s1, s2, s3;
    asm("s_nop 1\n\t"
        "v_dot2_f32_bf16 %0, %8, %18, 0\n\t"
        "v_dot2_f32_bf16 %1, %8, %19, 0\n\t"
        "v_dot2_f32_bf16 %2, %8, %20, 0\n\t"
        "v_dot2_f32_bf16 %3, %8, %21, 0\n\t"
        "v_fma_mix_f32 %4, %14, %16, 0 op_sel_hi:[1,0,0]\n\t"
        "v_fma_mix_f32 %5, %14, %16, 0 op_sel:[1,0,0] op_sel_hi:[1,0,0]\n\t"
        "v_fma_mix_f32 %6, %15, %16, 0 op_sel_hi:[1,0,0]\n\t"
        "v_fma_mix_f32 %7, %15, %16, 0 op_sel:[1,0,0] op_sel_hi:[1,0,0]\n\t"
        "v_dot2_f32_bf16 %0, %9, %22, %0\n\t"
        "v_dot2_f32_bf16 %1, %9, %23, %1\n\t"
        "v_dot2_f32_bf16 %2, %9, %24, %2\n\t"
        "v_dot2_f32_bf16 %3, %9, %25, %3\n\t"
        "v_dot2c_f32_f16 %4, %10, %17\n\t"
        "v_dot2c_f32_f16 %5, %11, %17\n\t"
        "v_dot2c_f32_f16 %6, %12, %17\n\t"
        "v_dot2c_f32_f16 %7, %13, %17\n\t"
        "v_fma_f32 %0, %0, %4, %26\n\t"
        "v_fma_f32 %1, %1, %5, %26\n\t"
        "v_fma_f32 %2, %2, %6, %26\n\t"
        "v_fma_f32 %3, %3, %7, %26\n\t"
        "v_lshl_or_b32 %0, %0, 8, 1\n\t"
        "v_lshlrev_b32 %1, 4, %1"
        : "=&v"(q0), "=&v"(q1), "=&v"(q2), "=&v"(q3), "=&v"(s0), "=&v"(s1), "=&v"(s2), "=&v"(s3)
        : "v"(g.x), "v"(g.y), "v"(ga.x), "v"(ga.y), "v"(ga.z), "v"(ga.w), "v"(gb.x), "v"(gb.y), "v"(c2), "v"(c01),
          "s"(k.wd[0]), "s"(k.wd[1]), "s"(k.wd[2]), "s"(k.wd[3]), "s"(k.wd[4]), "s"(k.wd[5]), "s"(k.wd[6]), "s"(k.wd[7]), "v"(k.magic));
    if (valid) {
        lds_add64(L_A0 + gr.l8, (u64)q0 | ((u64)q1 << 32));
        lds_add64(L_A1 + gr.l8, (u64)q2 | ((u64)q3 << 32));
    }
}
__device__ __forceinline__ void process(const Consts& k, u32 y, u32x2 g) {
    GRead gr = g_read(y);
    u32 c01, c01b; float c2, c2b;
    cos2(k, y, y, c01, c2, c01b, c2b);
    accum(k, gr, c01, c2b, g, true);
}

__device__ __forceinline__ void process_general(u32 y, u32x2 g) {
    GRead gr = g_read(y);
    const float a = bf_hi(y);
    float s[4] = {0.f, 0.f, 0.f, 0.f};
#pragma nounroll
    for (int l = 0; l < 3; l++) {
        float t = LDS(float, C_FR + l * 4) * a;
        float c = cosf(bf_lo(pk_bf16(t, t)));
#pragma unroll
        for (int d = 0; d < 4; d++) {
            u32 w = l < 2 ? gr.ga[d] >> (16 * l) : gr.gb[d >> 1] >> (16 * (d & 1));
            s[d] = __builtin_fmaf((float)__builtin_bit_cast(_Float16, (u16)w), c, s[d]);
        }
    }
#pragma unroll
    for (int d = 0; d < 4; d++) {
        float md = dot2bf(g.y, LDS(u32, C_WD + 16 + d * 4), dot2bf(g.x, LDS(u32, C_WD + d * 4), 0.f));
        (void)__hip_atomic_fetch_add(&LDS(double, L_F64 + d * (NLOC * 8) + gr.l8), (double)md * (double)s[d], __ATOMIC_RELAXED, __HIP_MEMORY_SCOPE_WORKGROUP);
    }
}

__device__ __forceinline__ u32 nunits(u32 d) { u32 o0 = d & 0xffff, o1 = d >> 16; return o1 > o0 ? (((o1 - 1) >> 1) - (o0 >> 1) + 1) : 0u; }

__device__ __forceinline__ u32 list_pos(u32 nu, u32& ta, u32& tb) {
    const int hh = threadIdx.x & 3, sh = 16 * (hh & 1);
    const u32 nc = min(nu, 4095u);
    u32 ia = wave_incl_scan(hh < 2 ? nc << sh : 0u), ib = wave_incl_scan(hh < 2 ? 0u : nc << sh);
    ta = __builtin_amdgcn_readlane(ia, 63); tb = __builtin_amdgcn_readlane(ib, 63);
    return (((hh < 2 ? ia : ib) >> sh) & 0xffff) - nc;
}

template <bool GENERAL>
__device__ __forceinline__ void direct(const Consts& k, u32 dsa, u32 dsb, u32 skip, u32 rec_stride, rsrc_t rR, rsrc_t rM, u32 rpb) {
    const u32 lane = threadIdx.x & 63;
    const u32 nu = nunits(dsa) + nunits(dsb);
    const u32 n = nu - min(skip, nu);
    const u32 inc = wave_incl_scan(n);
    const u32 tot = __builtin_amdgcn_readlane(inc, 63);
#pragma nounroll
    for (u32 t = lane; t - lane < tot; t += 64) {
        u32 o = 0;
#pragma unroll
        for (u32 st = 32; st > 0; st >>= 1) if (from_lane(inc, o + st - 1) <= t) o += st;
        const u32 da = from_lane(dsa, o), db = from_lane(dsb, o), j = t + from_lane(nu - inc, o);
        const u32 na = nunits(da);
        const bool inA = j < na, live = t < tot;
        const u32 d = inA ? da : db, u = ((d & 0xffff) >> 1) + (inA ? j : j - na);
        const u32 adr = (((threadIdx.x & ~63u) + o) >> 2) * rec_stride + (inA ? 0 : A_BYTES) + u * 12;
        u32x3 rr = __builtin_amdgcn_raw_buffer_load_b96(rR, live ? adr : (u32)OOB, 0, AUX_SC1);
#pragma nounroll
        for (u32 s = 0; s < 2; s++) {
            const u32 w = s ? rr.z : rr.x, idx = 2 * u + s;
            if (live && idx >= (d & 0xffff) && idx < (d >> 16)) {
                u32x2 g = __builtin_amdgcn_raw_buffer_load_b64(rM, ((w >> 24) * rpb + ((w >> 13) & 0x7ff)) * 8, 0, 0);
                u32 y = (w & 0xffff) | (s ? rr.y & 0xffff0000u : rr.y << 16);
                if (GENERAL) process_general(y, g); else process(k, y, g);
            }
        }
    }
}

__device__ __forceinline__ u32x2 rbf4(u32 dbits) {
    const u32x4 cc = LDS(u32x4, C_CEN + ((threadIdx.x >> 4) & 1) * 16);
    const float d = __uint_as_float(dbits << 16);
    u32x2 x;
#pragma unroll
    for (int q = 0; q < 2; q++) {
        u32 t = pk_bf16(d - __uint_as_float(cc[2 * q]), d - __uint_as_float(cc[2 * q + 1]));
        t = pk_bf16(bf_lo(t) * bf_lo(t), bf_hi(t) * bf_hi(t));
        t = pk_bf16(-10.f * bf_lo(t), -10.f * bf_hi(t));
        x[q] = pk_bf16(__expf(bf_lo(t)), __expf(bf_hi(t)));
    }
    if (d != d) x.x = ~0u;
    asm volatile("" : "+v"(x));
    return x;
}
template <int IT0, int IT1>
__device__ __forceinline__ void compute_g(const u32x2 (&xr)[4], int wave) {
    const int lane = threadIdx.x & 63, g = lane >> 4;
    const int j = (lane & 3) * 4 + ((lane >> 2) & 3);
    const u32x2 w = {LDS(u32, C_W + ((g & 1) * 32 + j) * 4), LDS(u32, C_W + ((g & 1) * 32 + 16 + j) * 4)};
    const u32x2 z = {0, 0};
    const s16x4 a0 = __builtin_bit_cast(s16x4, g < 2 ? w : z), a1 = __builtin_bit_cast(s16x4, g < 2 ? z : w);
#pragma unroll
    for (int it = IT0; it < IT1; it++) {
        const int tp = wave + 16 * it;
        u32x2 x = xr[it];
        const u64 nanm = __ballot(x.x == ~0u);
        if (nanm) { if (x.x == ~0u) x = z; }
#pragma unroll
        for (int t = 0; t < 2; t++) {
            f32x4 D = __builtin_amdgcn_mfma_f32_16x16x16bf16_1k(t ? a1 : a0, __builtin_bit_cast(s16x4, x), (f32x4){0.f, 0.f, 0.f, 0.f}, 0, 0, 0);
            float s = fmaxf(fabsf(D[0]) + fabsf(D[1]) + fabsf(D[2]), 1e-30f);
            u32 sb = (__float_as_uint(s) + 0xffffu) & 0xffff0000u;
            float inv = __builtin_amdgcn_rcpf(__uint_as_float(sb));
            const u32 o = (tp * 32 + t * 16 + (lane & 15)) * 16 + g * 4;
            u32 rb = pk_bf16(D[3], D[3]) & 0xffff;
            if (nanm) { if ((nanm >> (t * 32 + (lane & 15))) & 1) rb = 0x7fc0; }
            LDS(u32, L_GA + o) = pk_f16(D[0] * inv, D[1] * inv);
            LDS(_Float16, L_GB + o / 2) = (_Float16)(D[2] * inv);
            LDS(u32, L_SR + o) = sb | rb;
        }
    }
}

__global__ void __launch_bounds__(1024, 4) k_fused(const u32x2* __restrict__ m, const u32x4* __restrict__ idx_in, const u32x4* __restrict__ idx_out,
        const u16* __restrict__ dist, const u32x2* __restrict__ angle, const u16* __restrict__ centers, const u16* __restrict__ ang_freq,
        const u16* __restrict__ w_rbf, const u16* __restrict__ w_sbf, const u16* __restrict__ w_down, const u16* __restrict__ w_up,
        u32x2* __restrict__ out, u32* epoch, char* recs, char* dtab,
        int TQ, int E, int share_q, int rec_stride, int rows_per_wg, u32 MG, u32 rpb) {
    const int tid = threadIdx.x, lane = tid & 63;
    const int wave = __builtin_amdgcn_readfirstlane(tid >> 6);
    const int slot = blockIdx.x >> 3;
    const int b = (blockIdx.x & 7) * 32 + slot;
    u32 dv[4];
    u32x2 xr[4];
    u32 tag;
    {
        const int qbase = b * share_q;
        const int nq = max(0, min(share_q, TQ - qbase));
        u32x4 vo[QA + QB], vi[QA + QB], mv;
        u32x2 va[QA + QB];
        rsrc_t ro = mkrsrc(idx_out + qbase, nq * 16), ri = mkrsrc(idx_in + qbase, nq * 16), ra = mkrsrc(angle + qbase, nq * 8);
        const int r0 = b * rows_per_wg, nrows = max(0, min(rows_per_wg, E - r0));
        auto ldq = [&](int q) {
            vo[q] = __builtin_amdgcn_raw_buffer_load_b128(ro, tid * 16, q * 16384, AUX_NT);
            vi[q] = __builtin_amdgcn_raw_buffer_load_b128(ri, tid * 16, q * 16384, AUX_NT);
        };
        auto lda = [&](int q) { va[q] = __builtin_amdgcn_raw_buffer_load_b64(ra, tid * 8, q * 8192, AUX_NT); };
        ldq(0); ldq(1);
        pin();
        LDS(u32x2, B_CNT + tid * 8) = (u32x2){0, 0};
        if (tid < 4) LDS(u32, C_ST + tid * 4) = 0;
        u32 cw0, cw1;
        {
            const u16 *p0 = centers, *p1 = centers;
            if (wave == 15) {
                int rp = lane >> 4, j = lane & 15;
                p0 = j < 12 ? w_sbf + rp * 24 + j : w_rbf + rp * 8 + (j - 12);
                p1 = p0 + (j < 12 ? 12 : 4);
            } else if (wave == 14) {
                if (lane < 8) p1 = centers + lane;
                else if (lane < 16) { p0 = w_down + ((lane - 8) >> 2) * 8 + (lane & 3); p1 = p0 + 4; }
                else if (lane < 32) p1 = w_up + (lane - 16);
                else if (lane < 35) p1 = ang_freq + (lane - 32);
            }
            cw0 = *p0; cw1 = *p1;
            tag = __hip_atomic_load(epoch, __ATOMIC_RELAXED, __HIP_MEMORY_SCOPE_AGENT);
            if (wave == 0) {
                u64 pc;
                asm volatile("s_getpc_b64 %0" : "=s"(pc));
                l2_touch<B_DUMMY>(mkrsrc((const void*)(pc & ~255ull), CODE_PF), (lane * 4 + (slot & 3)) * 128);
            }
        }
        pin();
        lds_barrier();
        rsrc_t rd = mkrsrc(recs + (size_t)b * rec_stride, (nq * 24 + 15) & ~15);
        const int lim = (nq * 24) & ~15;
        auto copy_out = [&](int s) {
            int o = tid * 16 + s * 16384;
            u32x4 v = LDS(u32x4, B_STAGE + min(o, lim));
            __builtin_amdgcn_raw_buffer_store_b128(v, rd, o, 0, AUX_SC1);
        };
        const BinK bk = {MG, 2048 - rpb};
        u32 amax = 0;
        const u32 dA = bin_chunk<QA, 0>(vo, vi, va, min(nq, QA * 1024), bk, amax, [&](int s) {
            pin();
            if (s == 0 || s == 5) __builtin_amdgcn_s_setprio(3);
            if (s == 1 || s == 6) __builtin_amdgcn_s_setprio(2);
            if (s == 2 || s == 7) __builtin_amdgcn_s_setprio(1);
            if (s == 4 || s == 8) __builtin_amdgcn_s_setprio(0);
            if (s == 0) ldq(2);
            if (s == 1) ldq(3);
            if (s == 2) { lda(0); lda(1); lda(2); lda(3); }
            if (s == 3) {
                if (wave >= 14) {
                    LDS(u32, C_W + (15 - wave) * 256 + lane * 4) = ((wave == 15 || (lane >> 3) == 1) ? cw0 : 0u) | (cw1 << 16);
                    if (max(cw0 & 0x7fff, cw1 & 0x7fff) >= 0x7000) LDS(u32, C_ST + 12) = 1;
                }
                tag = __builtin_amdgcn_readfirstlane(tag) + 1;
            }
            if (s == 4) ldq(4);
            if (s == 5) ldq(5);
            if (s == 6) { ldq(6); lda(4); }
            if (s == 7) { lda(5); lda(6); }
            if (s == 8) {
                mv = __builtin_amdgcn_raw_buffer_load_b128(mkrsrc(m + r0, nrows * 8), tid * 16, 0, 0);
                rsrc_t rD = mkrsrc(dist, E * 2);
#pragma unroll
                for (int it = 0; it < 4; it++)
                    dv[it] = __builtin_amdgcn_raw_buffer_load_b16(rD, (((wave * 2 + (lane >> 5)) << 12) | (b << 4) | (lane & 15)) * 2, it * (32 << 13), 0);
            }
            pin();
        });
        const u32 dB = bin_chunk<QB, 1>(vo + QA, vi + QA, va + QA, max(nq - QA * 1024, 0), bk, amax, [&](int s) {
            pin();
            if (s == 100) copy_out(0);
            if (s == 101) copy_out(1);
            if (s == 4) { copy_out(2); copy_out(3); }
            if (s == 5) { copy_out(4); copy_out(5); }
            pin();
        });
        {
            u32x4 wa = LDS(u32x4, C_WD), wb = LDS(u32x4, C_WD + 16);
            u32 mmax = 0;
            float msum = 0.f;
#pragma unroll
            for (int j = 0; j < 2; j++)
#pragma unroll
                for (int d = 0; d < 4; d++) {
                    float x = fabsf(dot2bf(mv[2 * j + 1], wb[d], dot2bf(mv[2 * j], wa[d], 0.f)));
                    mmax = max(mmax, __float_as_uint(x) & 0x7fffffffu);
                    msum += x;
                }
            mmax = wave_max_u32(mmax);
            amax = wave_max_u32(max(amax & 0xffff, amax >> 16));
            msum = wave_sum_f32(msum);
            if (lane == 0) {
                __hip_atomic_fetch_max(&LDS(u32, C_ST), mmax, __ATOMIC_RELAXED, __HIP_MEMORY_SCOPE_WORKGROUP);
                __hip_atomic_fetch_max(&LDS(u32, C_ST + 4), amax, __ATOMIC_RELAXED, __HIP_MEMORY_SCOPE_WORKGROUP);
                __hip_atomic_fetch_add(&LDS(float, C_ST + 8), msum, __ATOMIC_RELAXED, __HIP_MEMORY_SCOPE_WORKGROUP);
            }
        }
        lds_barrier();
#pragma unroll
        for (int s = 6; s < 9; s++) copy_out(s);
        if (tid * 16 + 9 * 16384 <= lim) copy_out(9);
        const u32 mexp = LDS(u32, C_ST) >> 23;
        const u32 mean_exp = (__float_as_uint(LDS(float, C_ST + 8) / (float)(4 * max(nrows, 1))) >> 23) & 255;
        const u32 st2 = (tid & 1) ? (nrows ? 255 - mean_exp : 0u) : LDS(u32, C_ST + 4) >> 7;
        LDS(u32x4, L_A0 + tid * 16) = (u32x4){0, 0, 0, 0};
        if (tid < NLOC - 1024) LDS(u32x4, L_A0 + 16384 + tid * 16) = (u32x4){0, 0, 0, 0};
        xr[0] = rbf4(dv[0]); xr[1] = rbf4(dv[1]); xr[2] = rbf4(dv[2]);
        asm volatile("s_waitcnt vmcnt(0) lgkmcnt(0)\n\ts_barrier" ::: "memory");
        __builtin_amdgcn_raw_buffer_store_b128((u32x4){dA, (tag << 8) | mexp, dB, (tag << 8) | st2}, mkrsrc(dtab, NB * NB * 64),
                                               (tid >> 2) * (NB * 64) + b * 64 + (tid & 3) * 16, 0, AUX_SC1);
        __builtin_amdgcn_s_setprio(3);
        compute_g<0, 3>(xr, wave);
    }
    const u32 cur0 = lane < NBIN ? lane * (BSTR * 8) : 0;
    if (tid < 64) LDS(u32, L_CNT + tid * 4) = cur0;
    rsrc_t rDt = mkrsrc(dtab + (size_t)b * (NB * 64), NB * 64);
    rsrc_t rR = mkrsrc(recs, NB * rec_stride);
    rsrc_t rM = mkrsrc(m, E * 8);
    Consts k;
#pragma unroll
    for (int i = 0; i < 3; i++) k.fr[i] = LDS(float, C_FR + i * 4);
#pragma unroll
    for (int i = 0; i < 8; i++) k.wd[i] = __builtin_amdgcn_readfirstlane(LDS(u32, C_WD + i * 4));
    const u32x3 fb = LDS(u32x3, C_FR);
    const int fexp = __builtin_amdgcn_readfirstlane(max(max(fb.x & 0x7fffffffu, fb.y & 0x7fffffffu), fb.z & 0x7fffffffu) >> 23);
    const bool wild = __builtin_amdgcn_readfirstlane(LDS(u32, C_ST + 12));
    asm volatile("" : "+v"(k.fr[0]), "+v"(k.fr[1]), "+v"(k.fr[2]));
    const int hrot = (slot >> 3) & 3;
    const u32 doff = (tid & ~3) * 16 + ((tid + hrot) & 3) * 16;
    u32x4 dsc;
    {
        u32 spins = 0;
        bool ok = false;
        __builtin_amdgcn_s_sleep(12);
        for (;;) {
            if (!ok) {
                dsc = __builtin_amdgcn_raw_buffer_load_b128(rDt, doff, 0, AUX_SC1);
                ok = ((dsc.y ^ (tag << 8)) | (dsc.w ^ (tag << 8))) < 256u;
            }
            if (__all(ok)) break;
            if (++spins > (1u << 16)) { dsc = (u32x4){0, 0, 0, 0}; break; }
            __builtin_amdgcn_s_sleep(10);
        }
    }
    __builtin_amdgcn_fence(__ATOMIC_ACQUIRE, "wavefront");
    u32x3 r[H][4];
    u32 fl = 0;
    bool leftover;
    u32 en[H][4];
    auto ld = [&](int u) {
#pragma unroll
        for (int h = 0; h < H; h++) r[h][u] = __builtin_amdgcn_raw_buffer_load_b96(rR, en[h][u], 0, AUX_SC1);
    };
    {
        const u32 dsa = dsc.x, dsb = dsc.z;
        const u32 nua = nunits(dsa), nu = nua + nunits(dsb);
        u32 ta, tb;
        const u32 p = list_pos(nu, ta, tb);
        const u32 utot[H] = {ta & 0xffff, ta >> 16, tb & 0xffff, tb >> 16};
        leftover = max(max(utot[0], utot[1]), max(utot[2], utot[3])) > 256;
        const u32 lbase = L_STAGE + wave * 4096;
        u32 lp = lbase + (tid & 3) * 1024 + p * 4;
        u32 room = 256 - min(p, 256u);
        auto emit = [&](u32 d, u32 nall, u32 base) {
            u32 n = min(nall, room);
            room -= n;
            if (n) {
                u32 e0 = base + ((d & 0xffff) >> 1) * 12;
                u32 lend = lp + n * 4 - 4;
                u32 e = e0;
#pragma nounroll
                for (u32 q = lp; q < lend; q += 8) { LDS(u32, q) = e; LDS(u32, q + 4) = e + 12; e += 24; }
                u32 fl_last = n == nall ? (d << 14) & 0x40000000u : 0u;
                LDS(u32, lend) = (e0 + (n - 1) * 12) | fl_last;
                LDS(u32, lp) = e0 | (d << 31) | (n == 1 ? fl_last : 0u);
                lp = lend + 4;
            }
        };
        emit(dsa, nua, (u32)(tid >> 2) * rec_stride);
        emit(dsb, nu - nua, (u32)(tid >> 2) * rec_stride + A_BYTES);
        lds_wait();
#pragma unroll
        for (int h = 0; h < H; h++)
#pragma unroll
            for (int u = 0; u < 4; u++) {
                u32 e = LDS(u32, lbase + h * 1024 + u * 256 + lane * 4);
                e = (u32)(lane + u * 64) < utot[h] ? e : ~0u;
                fl |= (e >> 30) << (30 - 8 * h - 2 * u);
                en[h][u] = e & 0x3fffffffu;
            }
        __builtin_amdgcn_s_setprio(0);
        ld(0);
        pin();
        {
            u32 s0 = wave_max_u32(dsc.y & 255), s1 = wave_max_u32((doff & 16) ? 0u : dsc.w & 255), s2 = wave_max_u32((doff & 16) ? dsc.w & 255 : 0u);
            u32 nrec = wave_incl_scan((dsa >> 16) - (dsa & 0xffff) + (dsb >> 16) - (dsb & 0xffff));
            if (lane == 63) LDS(u32x4, L_MISC + wave * 16) = (u32x4){s0, s1, s2, nrec};
        }
    }
    lds_barrier();
    __builtin_amdgcn_s_setprio(3);
    ld(1);
    __builtin_amdgcn_s_setprio(2);
    pin();
    if (blockIdx.x == 0 && tid == 0) __hip_atomic_store(epoch, tag, __ATOMIC_RELAXED, __HIP_MEMORY_SCOPE_AGENT);
    int ex, nrec_total;
    bool general;
    {
        u32x4 t = LDS(u32x4, L_MISC + (lane & 15) * 16);
        ex = (int)wave_max_u32(t.x) + 1;
        const int aexp = wave_max_u32(t.y), mean_exp = 255 - (int)wave_max_u32(t.z);
        nrec_total = __builtin_amdgcn_readlane(row_incl_scan(t.w), 15);
        general = ex > 240 || ex < 30 || ex - mean_exp > 8 || aexp == 255 || aexp + fexp > 260 || wild;
        if (general) ex = 127;
    }
    const u32 magic_bits = ((u32)(ex + 4) << 23) | 0x400000u;
    k.magic = __uint_as_float(magic_bits);
    asm volatile("" : "+v"(k.fr[0]), "+v"(k.fr[1]), "+v"(k.fr[2]), "+v"(k.magic));
    const float Kinv = __uint_as_float((u32)(ex - 19) << 23);

    auto rank = [&](u32x3 rr, u32 f, u32& k0, u32& k1) {
        const u32 INC = (1u << 18) | 8u;
        k0 = 0; k1 = 0;
        if ((int)f >= 0) k0 = lds_add_rtn(L_CNT + ((rr.x >> 24) & 0xfc), INC);
        if ((int)(f << 1) >= 0) k1 = lds_add_rtn(L_CNT + ((rr.z >> 24) & 0xfc), INC);
    };
    auto put = [&](u32x3 rr, u32 f, u32 k0, u32 k1) {
        if (__builtin_expect(__any((k0 | k1) >= (128u << 18)), 0)) {
            if (k0 >= (128u << 18)) k0 = (NBIN * BSTR + min(lds_add_rtn(L_CNT + 252, 1u), (u32)OCAP - 1)) * 8;
            if (k1 >= (128u << 18)) k1 = (NBIN * BSTR + min(lds_add_rtn(L_CNT + 252, 1u), (u32)OCAP - 1)) * 8;
        }
        if ((int)f >= 0) LDS(u32x2, L_STAGE + (k0 & 0x3ffff)) = (u32x2){rr.x, __builtin_amdgcn_perm(rr.y, rr.x, 0x05040100)};
        if ((int)(f << 1) >= 0) LDS(u32x2, L_STAGE + (k1 & 0x3ffff)) = (u32x2){rr.z, __builtin_amdgcn_perm(rr.y, rr.z, 0x07060100)};
    };
    auto place = [&](u32x3 rr, u32 f) { u32 k0, k1; rank(rr, f, k0, k1); put(rr, f, k0, k1); };

    if (general) fl = ~0u;
    auto ld1 = [&](int h, int u) { r[h][u] = __builtin_amdgcn_raw_buffer_load_b96(rR, en[h][u], 0, AUX_SC1); };
    place(r[0][0], fl);
    pin();
    ld1(0, 2); ld1(0, 3); ld1(1, 2); ld1(1, 3);
    __builtin_amdgcn_s_setprio(1);
    pin();
    place(r[0][1], fl << 2);
    if (wave == 0) { xr[3] = rbf4(dv[3]); compute_g<3, 4>(xr, wave); }
    place(r[0][2], fl << 4); place(r[0][3], fl << 6);
    pin();
    ld1(2, 2); ld1(3, 2); ld1(2, 3); ld1(3, 3);
    __builtin_amdgcn_s_setprio(0);

    u32 y[8];
    u32x2 g[8];
#pragma unroll
    for (int i = 0; i < 8; i++) { y[i] = ~0u; g[i] = (u32x2){0, 0}; }
    const int blk0 = (wave & 1) ? 7 * wave + 6 : 7 * wave, bstep = (wave & 1) ? -1 : 1;
    auto proc = [&](int i) { if (y[i] != ~0u) process(k, y[i], g[i]); };
#pragma nounroll
    for (int h = 0; h < H; h++) {
        fl = (fl << 8) | 0xff;
        lds_barrier();
        __builtin_amdgcn_s_setprio(3);
        u32x2 x[8];
        if (tid < 64) { u32 c = LDS(u32, L_CNT + tid * 4); LDS(u32, L_CNT + tid * 4) = cur0; LDS(u32, L_FIN + tid * 4) = tid == 63 ? min(c, (u32)OCAP) : c >> 18; }
#pragma unroll
        for (int i = 0; i < 7; i++) { int blk = blk0 + i * bstep; x[i] = LDS(u32x2, L_STAGE + ((blk >> 1) * BSTR + (blk & 1) * 64) * 8 + lane * 8); }
        x[7] = LDS(u32x2, L_STAGE + NBIN * BSTR * 8 + tid * 8);
        lds_barrier();
        const u32 cn = LDS(u32, L_FIN + lane * 4);
        auto gather = [&](int i) {
            int blk = blk0 + i * bstep;
            int nv = i < 7 ? (int)__builtin_amdgcn_readlane(cn, blk >> 1) - (blk & 1) * 64 : (int)__builtin_amdgcn_readlane(cn, 63) - wave * 64;
            u32 o = (x[i].x >> 10) & 0x3ff8u;
            if (i == 7) o += mul24(x[i].x >> 24, rpb * 8);
            const bool v = lane < nv;
            g[i] = __builtin_amdgcn_raw_buffer_load_b64(rM, v ? o : (u32)OOB, i < 7 ? ((blk >> 1) * 4 + ((h + hrot) & 3)) * rpb * 8 : 0, 0);
            y[i] = v ? x[i].y : ~0u;
        };
        auto step = [&](int q, u32x3 rr, u32 f) {
            u32 k0, k1;
            rank(rr, f, k0, k1);
            GRead ra = g_read(y[2 * q]), rb = g_read(y[2 * q + 1]);
            pin();
            u32 ca, cb; float da, db;
            cos2(k, y[2 * q], y[2 * q + 1], ca, da, cb, db);
            asm volatile("" : "+v"(ca), "+v"(cb), "+v"(da), "+v"(db));
            put(rr, f, k0, k1);
            accum(k, ra, ca, da, g[2 * q], y[2 * q] != ~0u);
            accum(k, rb, cb, db, g[2 * q + 1], y[2 * q + 1] != ~0u);
            pin();
        };
        y[7] = ~0u;
        if (__builtin_amdgcn_readlane(cn, 63) > (u32)wave * 64) gather(7);
        gather(6);
        pin();
        step(2, r[1][0], fl);
        gather(0); gather(1);
        pin();
        __builtin_amdgcn_s_setprio(2);
        place(r[1][1], fl << 2);
        proc(7); proc(6);
        pin();
        __builtin_amdgcn_s_setprio(1);
        step(0, r[1][2], fl << 4);
        gather(2); gather(3);
        pin();
        __builtin_amdgcn_s_setprio(0);
        step(1, r[1][3], fl << 6);
        gather(4); gather(5);
        pin();
#pragma unroll
        for (int u = 0; u < 4; u++) { r[1][u] = r[2][u]; r[2][u] = r[3][u]; }
    }
    u32x2 mrow[2];
#pragma unroll
    for (int j = 0; j < 2; j++) {
        int loc = tid + j * 1024;
        int e = ((loc >> 4) << 12) | (b << 4) | (loc & 15);
        mrow[j] = __builtin_amdgcn_raw_buffer_load_b64(rM, e * 8, 0, 0);
    }
    pin();
    proc(4); proc(5);
    if (__builtin_expect(leftover && !general, 0)) {
        u32x4 d = __builtin_amdgcn_raw_buffer_load_b128(rDt, doff, 0, AUX_SC1);
        u32 ta, tb;
        const u32 p = list_pos(nunits(d.x) + nunits(d.z), ta, tb);
        direct<false>(k, d.x, d.z, 256 - min(p, 256u), rec_stride, rR, rM, rpb);
    }
    if (tid == 0) LDS(u32, L_MISC) = 0;
    f32x4 wu[4];
    u32x4 sr[2];
#pragma unroll
    for (int i = 0; i < 4; i++) wu[i] = LDS(f32x4, C_WU + i * 16);
#pragma unroll
    for (int j = 0; j < 2; j++) sr[j] = LDS(u32x4, L_SR + min(tid + j * 1024, NLOC - 1) * 16);
    lds_barrier();
    auto finish = [&](int j, const float (&f)[4]) {
        int loc = tid + j * 1024;
        int e = ((loc >> 4) << 12) | (b << 4) | (loc & 15);
        float mm[4] = {bf_lo(mrow[j].x), bf_hi(mrow[j].x), bf_lo(mrow[j].y), bf_hi(mrow[j].y)};
        float o[4], ag[4];
#pragma unroll
        for (int d = 0; d < 4; d++) ag[d] = f[d] * bf_hi(sr[j][d]);
#pragma unroll
        for (int d = 0; d < 4; d++) {
            float u = ag[0] * wu[0][d] + ag[1] * wu[1][d] + ag[2] * wu[2][d] + ag[3] * wu[3][d];
            o[d] = mm[d] + u * bf_lo(sr[j][d]);
        }
        if (e < E) out[e] = (u32x2){pk_bf16(o[0], o[1]), pk_bf16(o[2], o[3])};
    };
    u32 nsum = 0;
    {
        const u64 c0 = 0 - ((u64)((magic_bits << 8) | 1u) | ((u64)(magic_bits << 4) << 32));
        const u64 c1 = 0 - ((u64)magic_bits | ((u64)magic_bits << 32));
        u64 w0[2], w1[2];
#pragma unroll
        for (int j = 0; j < 2; j++) { int loc = min(tid + j * 1024, NLOC - 1); w0[j] = LDS(u64, L_A0 + loc * 8); w1[j] = LDS(u64, L_A1 + loc * 8); }
        nsum = ((u32)w0[0] & 255) + (tid + 1024 < NLOC ? (u32)w0[1] & 255 : 0u);
        nsum = wave_incl_scan(nsum);
        if (lane == 63) lds_add_rtn(L_MISC, nsum);
#pragma unroll
        for (int j = 0; j < 2; j++) {
            if (tid + j * 1024 >= NLOC) break;
            const u32 n = (u32)w0[j] & 255;
            u64 a = w0[j] + (u64)n * (u32)c0, bq = w1[j] + (u64)n * (u32)c1;
            u32 alo = (u32)a, ahi = (u32)(a >> 32) + n * (u32)(c0 >> 32), blo = (u32)bq, bhi = (u32)(bq >> 32) + n * (u32)(c1 >> 32);
            int f0 = (int)__builtin_amdgcn_alignbit(ahi, alo, 4) >> 4;
            int f1 = ((int)ahi >> 4) - (f0 >> 31);
            int f3 = (int)bhi - ((int)blo >> 31);
            float ag[4] = {(float)f0 * Kinv, (float)f1 * Kinv, (float)(int)blo * Kinv, (float)f3 * Kinv};
            finish(j, ag);
        }
    }
    lds_barrier();
    if (__builtin_expect((int)LDS(u32, L_MISC) != nrec_total, 0)) {
        for (int i = tid; i < NLOC * 4; i += 1024) LDS(u64, L_F64 + i * 8) = 0;
        lds_barrier();
        u32x4 d = __builtin_amdgcn_raw_buffer_load_b128(rDt, doff, 0, AUX_SC1);
        if (((d.y ^ (tag << 8)) | (d.w ^ (tag << 8))) >= 256u) d = (u32x4){0, 0, 0, 0};
        direct<true>(k, d.x, d.z, 0, rec_stride, rR, rM, rpb);
        lds_barrier();
        asm volatile("s_waitcnt vmcnt(0)" ::: "memory");
#pragma unroll
        for (int j = 0; j < 2; j++) {
            int loc = tid + j * 1024;
            if (loc >= NLOC) break;
            float ag[4];
#pragma unroll
            for (int d4 = 0; d4 < 4; d4++) ag[d4] = (float)LDS(double, L_F64 + d4 * (NLOC * 8) + loc * 8);
            finish(j, ag);
        }
    }
}

__device__ __forceinline__ float bfv(u16 v) { return __uint_as_float((u32)v << 16); }
__device__ __forceinline__ float rnd(float x) { return bf_lo(pk_bf16(x, x)); }
__device__ __forceinline__ float rbf_of(float d, float c) { float t = rnd(d - c); return rnd(__expf(rnd(-10.f * rnd(t * t)))); }

__global__ void k_zero(float* agg, int n) {
    int i = blockIdx.x * 256 + threadIdx.x;
    if (i < n) agg[i] = 0.f;
}
__global__ void k_triplets(const u16* m, const int* idx_in, const int* idx_out, const u16* dist, const u16* angle, const u16* centers,
                           const u16* ang_freq, const u16* w_sbf, const u16* w_down, float* agg, int T) {
    int t = blockIdx.x * 256 + threadIdx.x;
    if (t >= T) return;
    const size_t ei = idx_in[t], eo = idx_out[t];
    const float a = bfv(angle[t]), d = bfv(dist[eo]);
    float c0 = cosf(rnd(bfv(ang_freq[0]) * a)), c1 = cosf(rnd(bfv(ang_freq[1]) * a)), c2 = cosf(rnd(bfv(ang_freq[2]) * a));
    float s[4] = {0.f, 0.f, 0.f, 0.f};
    for (int r = 0; r < 8; r++) {
        float rb = rbf_of(d, bfv(centers[r]));
#pragma unroll
        for (int c = 0; c < 4; c++) s[c] += rb * (c0 * bfv(w_sbf[r * 12 + c]) + c1 * bfv(w_sbf[r * 12 + 4 + c]) + c2 * bfv(w_sbf[r * 12 + 8 + c]));
    }
#pragma unroll
    for (int c = 0; c < 4; c++) {
        float md = 0.f;
#pragma unroll
        for (int q = 0; q < 4; q++) md += bfv(m[ei * 4 + q]) * bfv(w_down[q * 4 + c]);
        atomicAdd(&agg[eo * 4 + c], md * s[c]);
    }
}
__global__ void k_edges(const u16* m, const u16* dist, const u16* centers, const u16* w_rbf, const u16* w_up, const float* agg, u16* out, int E) {
    int e = blockIdx.x * 256 + threadIdx.x;
    if (e >= E) return;
    const float d = bfv(dist[e]);
    float g[4] = {0.f, 0.f, 0.f, 0.f};
    for (int r = 0; r < 8; r++) {
        float rb = rbf_of(d, bfv(centers[r]));
#pragma unroll
        for (int c = 0; c < 4; c++) g[c] += rb * bfv(w_rbf[r * 4 + c]);
    }
#pragma unroll
    for (int c = 0; c < 4; c++) {
        float u = 0.f;
#pragma unroll
        for (int q = 0; q < 4; q++) u += agg[(size_t)e * 4 + q] * bfv(w_up[q * 4 + c]);
        float o = bfv(m[(size_t)e * 4 + c]) + u * g[c];
        out[(size_t)e * 4 + c] = (u16)pk_bf16(o, o);
    }
}

extern "C" void kernel_launch(void* const* d_in, const int* in_sizes, int n_in,
                              void* d_out, int out_size, void* d_ws, size_t ws_size,
                              hipStream_t stream) {
    const int E = in_sizes[3], T = in_sizes[1];
    const int TQ = T / 4;
    const int share_q = ((TQ + NB - 1) / NB + 15) & ~15;
    const int rec_stride = (share_q * 24 + 127) / 128 * 128;
    int dev = 0, cus = 0;
    (void)hipGetDevice(&dev);
    (void)hipDeviceGetAttribute(&cus, hipDeviceAttributeMultiprocessorCount, dev);
    uintptr_t al = (uintptr_t)d_in[0] | (uintptr_t)d_in[1] | (uintptr_t)d_in[2] | (uintptr_t)d_in[4] | (uintptr_t)d_out | (uintptr_t)d_ws;
    const bool fused = cus >= NB && E > 0 && T > 0 && E <= NLOC * NB && (E & 1) == 0 && (T & 3) == 0 && B_STAGE + rec_stride <= C_ST &&
                       (al & 15) == 0 && ws_size >= (48u << 20);
    char* ws = (char*)d_ws;
    if (!fused) {
        float* agg = (float*)ws;
        if (E <= 0) return;
        k_zero<<<(E * 4 + 255) / 256, 256, 0, stream>>>(agg, E * 4);
        if (T > 0) k_triplets<<<(T + 255) / 256, 256, 0, stream>>>((const u16*)d_in[0], (const int*)d_in[1], (const int*)d_in[2], (const u16*)d_in[3],
            (const u16*)d_in[4], (const u16*)d_in[5], (const u16*)d_in[6], (const u16*)d_in[8], (const u16*)d_in[9], agg, T);
        k_edges<<<(E + 255) / 256, 256, 0, stream>>>((const u16*)d_in[0], (const u16*)d_in[3], (const u16*)d_in[5], (const u16*)d_in[7],
            (const u16*)d_in[10], agg, (u16*)d_out, E);
        return;
    }
    u32* epoch = (u32*)ws;
    char* recs = ws + (1 << 20);
    char* dtab = ws + (40u << 20);
    const int rows = ((E + NB - 1) / NB + 15) & ~15;
    const u32 nlines = (E + 15) / 16;
    const u32 lpb = (nlines + H * NBIN - 1) / (H * NBIN);
    const u32 MG = ((1u << 22) + lpb - 1) / lpb;
    (void)hipFuncSetAttribute((const void*)k_fused, hipFuncAttributeMaxDynamicSharedMemorySize, LDS_BYTES);
    k_fused<<<NB, 1024, LDS_BYTES, stream>>>((const u32x2*)d_in[0], (const u32x4*)d_in[1], (const u32x4*)d_in[2], (const u16*)d_in[3],
        (const u32x2*)d_in[4], (const u16*)d_in[5], (const u16*)d_in[6], (const u16*)d_in[7], (const u16*)d_in[8], (const u16*)d_in[9],
        (const u16*)d_in[10], (u32x2*)d_out, epoch, recs, dtab, TQ, E, share_q, rec_stride, rows, MG, lpb * 16);
}
```
